```python
import torch

def _softmax(x, axis=-1):
    return torch.softmax(x, dim=axis)
def _gelu(x):
    return torch.nn.functional.gelu(x, approximate="tanh")
def _silu(x):
    return torch.nn.functional.silu(x)
def _sigmoid(x):
    return torch.sigmoid(x)
def _layernorm(x, axis=-1, eps=1e-5):
    m = x.mean(dim=axis, keepdim=True)
    v = x.var(dim=axis, keepdim=True, unbiased=False)
    return (x - m) / torch.sqrt(v + eps)
def _scatter_add(tgt, idx, src, axis=0):
    return tgt.clone().index_add_(axis, idx, src)
def _segment_sum(src, seg, n):
    out = torch.zeros((n,) + src.shape[1:], dtype=src.dtype)
    return out.index_add_(0, seg, src)
def _pad(x, pw):
    flat = tuple(v for pair in reversed(pw) for v in pair)
    return torch.nn.functional.pad(x, flat)

kernel_name = 'sequential_preLN__gqa_block_cached__dense_activation_squared_relu__decode'

def setup_inputs():

    _w = (lambda a: (lambda t: t.to(torch.bfloat16) if t.is_floating_point() else t)(torch.as_tensor(a)))
    def _A_setup():
        B, Sq, D = 8, 1, 448
        Hq, Hkv, Sk, Dh = 16, 8, 4096, 256
        assert Hq % Hkv == 0, (Hq, Hkv)
        return {
            "x": _w((lambda *s: torch.randn(*s, dtype=torch.bfloat16) * 0.05)(B, Sq, D)),
            "k_cache": _w((lambda *s: torch.randn(*s, dtype=torch.bfloat16) * 3.2)(B, Hkv, Sk, Dh)),
            "v_cache": _w((lambda *s: torch.randn(*s, dtype=torch.bfloat16) * 0.4)(B, Hkv, Sk, Dh)),
            "wq": _w((lambda *s: torch.randn(*s, dtype=torch.bfloat16) * 3.2)(D, Hq * Dh) * (D ** -0.5)),
            "wo": _w((lambda *s: torch.randn(*s, dtype=torch.bfloat16) * 0.4)(Hq * Dh, D) * ((Hq * Dh) ** -0.5)),
        }
    _A_in = _A_setup()

    def _B_setup():
        B, S, D, F = 8, 1, 448, 1792
        return {
            "x": _w((lambda *s: torch.randn(*s, dtype=torch.bfloat16) * 0.05)(B, S, D)),
            "w1": _w((lambda *s: torch.randn(*s, dtype=torch.bfloat16) * 0.05)(D, F)),
            "b1": _w((lambda *s: torch.randn(*s, dtype=torch.bfloat16) * 0.8)(F)),
            "w2": _w((lambda *s: torch.randn(*s, dtype=torch.bfloat16) * 0.05)(F, D)),
            "b2": _w((lambda *s: torch.randn(*s, dtype=torch.bfloat16) * 3.2)(D)),
        }
    _B_in = _B_setup()

    return {
        "A_x": _A_in["x"],
        "A_k_cache": _A_in["k_cache"],
        "A_v_cache": _A_in["v_cache"],
        "A_wq": _A_in["wq"],
        "A_wo": _A_in["wo"],
        "B_x": _B_in["x"],
        "B_w1": _B_in["w1"],
        "B_b1": _B_in["b1"],
        "B_w2": _B_in["w2"],
        "B_b2": _B_in["b2"],
    }

def reference(A_x, A_k_cache, A_v_cache, A_wq, A_wo, B_x, B_w1, B_b1, B_w2, B_b2):
    def _A_ref(x, k_cache, v_cache, wq, wo):
        B, Sq, D = x.shape
        Hq, Dh = 16, 256
        Hkv = k_cache.shape[1]
        rep = Hq // Hkv
        q = (lambda x, *d: x.permute(*d))((x @ wq).reshape(B, Sq, Hq, Dh), 0, 2, 1, 3)
        k = (lambda x, n, axis: torch.repeat_interleave(x, n, dim=axis))(k_cache, rep, axis=1)
        v = (lambda x, n, axis: torch.repeat_interleave(x, n, dim=axis))(v_cache, rep, axis=1)
        scores = torch.einsum("bhqd,bhkd->bhqk", q, k) * (Dh ** -0.5)
        p = _softmax(scores, axis=-1)
        o = torch.einsum("bhqk,bhkd->bhqd", p, v)
        return (lambda x, *d: x.permute(*d))(o, 0, 2, 1, 3).reshape(B, Sq, Hq * Dh) @ wo

    def _B_ref(x, w1, b1, w2, b2):
        h = torch.einsum("bsd,df->bsf", x, w1) + b1
        zero = torch.zeros_like(h)
        r = torch.where(h > zero, h, zero)
        h = r * r
        out = torch.einsum("bsf,fd->bsd", h, w2) + b2
        return out

    x = A_x
    h = x + _A_ref(_layernorm(x, axis=-1), A_k_cache, A_v_cache, A_wq, A_wo)
    h2 = h + _B_ref(_layernorm(h, axis=-1), B_w1, B_b1, B_w2, B_b2)
    return h2
```

```cpp
#include <hip/hip_runtime.h>
#include <hip/hip_bf16.h>
#include <stdint.h>

typedef unsigned v4u __attribute__((ext_vector_type(4)));
typedef unsigned v2u __attribute__((ext_vector_type(2)));
typedef float f32x4 __attribute__((ext_vector_type(4)));
typedef short bf16x8 __attribute__((ext_vector_type(8)));
typedef __bf16 bf16x2 __attribute__((ext_vector_type(2)));

constexpr int B_ = 8, D_ = 448, HQ = 16, HKV = 8, SK = 4096, DH = 256, F_ = 1792;

__device__ __forceinline__ float bf2f(unsigned short u) { return __builtin_bit_cast(float, (unsigned)u << 16); }
typedef float f32x2 __attribute__((ext_vector_type(2)));
__device__ __forceinline__ unsigned cvtpk(float lo, float hi) { f32x2 v = {lo, hi}; return __builtin_bit_cast(unsigned, __builtin_convertvector(v, bf16x2)); }
__device__ __forceinline__ float rbf(float x) { return __builtin_bit_cast(float, cvtpk(x, x) << 16); }
__device__ __forceinline__ unsigned short f2bf(float x) { return (unsigned short)(cvtpk(x, x) & 0xffffu); }
__device__ __forceinline__ float dot2(unsigned a, unsigned b, float c) {
  return __builtin_amdgcn_fdot2_f32_bf16(__builtin_bit_cast(bf16x2, a), __builtin_bit_cast(bf16x2, b), c, false);
}
template <int CTRL> __device__ __forceinline__ float dppf(float x) {
  return __builtin_bit_cast(float, __builtin_amdgcn_mov_dpp(__builtin_bit_cast(int, x), CTRL, 0xf, 0xf, true));
}
template <int CTRL> __device__ __forceinline__ unsigned dppu(unsigned x) {
  return (unsigned)__builtin_amdgcn_mov_dpp((int)x, CTRL, 0xf, 0xf, true);
}
constexpr int XOR1 = 0xB1, XOR2 = 0x4E, XOR7 = 0x141, XOR8 = 0x128;
__device__ __forceinline__ float xrow_max(float x) {
  auto s = __builtin_amdgcn_permlane16_swap(__float_as_uint(x), __float_as_uint(x), false, false);
  x = fmaxf(__uint_as_float(s[0]), __uint_as_float(s[1]));
  auto t = __builtin_amdgcn_permlane32_swap(__float_as_uint(x), __float_as_uint(x), false, false);
  return fmaxf(__uint_as_float(t[0]), __uint_as_float(t[1]));
}
__device__ __forceinline__ float xrow_sum(float x) {
  auto s = __builtin_amdgcn_permlane16_swap(__float_as_uint(x), __float_as_uint(x), false, false);
  x = __uint_as_float(s[0]) + __uint_as_float(s[1]);
  auto t = __builtin_amdgcn_permlane32_swap(__float_as_uint(x), __float_as_uint(x), false, false);
  return __uint_as_float(t[0]) + __uint_as_float(t[1]);
}
__device__ __forceinline__ float wave_max(float x) {
  x = fmaxf(x, dppf<XOR1>(x)); x = fmaxf(x, dppf<XOR2>(x)); x = fmaxf(x, dppf<XOR7>(x)); x = fmaxf(x, dppf<XOR8>(x));
  return xrow_max(x);
}
__device__ __forceinline__ float wave_sum(float x) {
  x += dppf<XOR1>(x); x += dppf<XOR2>(x); x += dppf<XOR7>(x); x += dppf<XOR8>(x);
  return xrow_sum(x);
}
__device__ __forceinline__ f32x4 mfma16(v4u a, v4u b, f32x4 c) {
  return __builtin_amdgcn_mfma_f32_16x16x32_bf16(__builtin_bit_cast(bf16x8, a), __builtin_bit_cast(bf16x8, b), c, 0, 0, 0);
}

__device__ __forceinline__ void ln_row(const float (&v)[7], float (&o)[7]) {
  float s = 0.f;
#pragma unroll
  for (int i = 0; i < 7; ++i) s += v[i];
  s = wave_sum(s);
  const float mean = s * (1.f / 448.f);
  float q = 0.f;
#pragma unroll
  for (int i = 0; i < 7; ++i) { float d = v[i] - mean; q += d * d; }
  q = wave_sum(q);
  const float m = rbf(mean), var = rbf(q * (1.f / 448.f));
  const float den = rbf(sqrtf(rbf(var + 1e-5f)));
#pragma unroll
  for (int i = 0; i < 7; ++i) o[i] = rbf(rbf(v[i] - m) / den);
}

__global__ void __launch_bounds__(512) k_qproj(const unsigned short* x, const unsigned short* wq, unsigned short* q) {
  __shared__ float xn[8][448];
  __shared__ float red[4][128];
  const int tid = threadIdx.x, lane = tid & 63, w = tid >> 6;
  float v[7], o[7];
#pragma unroll
  for (int i = 0; i < 7; ++i) v[i] = bf2f(x[w * 448 + lane + 64 * i]);
  ln_row(v, o);
#pragma unroll
  for (int i = 0; i < 7; ++i) xn[w][lane + 64 * i] = o[i];
  __syncthreads();
  const int n = tid & 15, b = (tid >> 4) & 7, kq = tid >> 7;
  const int col = blockIdx.x * 16 + n;
  float acc = 0.f;
  for (int k = kq * 112; k < kq * 112 + 112; ++k) acc += xn[b][k] * bf2f(wq[(size_t)k * 4096 + col]);
  red[kq][tid & 127] = acc;
  __syncthreads();
  if (tid < 128) q[b * 4096 + col] = f2bf(red[0][tid] + red[1][tid] + red[2][tid] + red[3][tid]);
}

constexpr int RING = 32;
__global__ void __launch_bounds__(256) k_attn(const unsigned short* Kc, const unsigned short* Vc, const unsigned short* q, float* pml, float* po) {
  const int tid = threadIdx.x, lane = tid & 63, w = __builtin_amdgcn_readfirstlane(tid >> 6);
  const int bid = blockIdx.x, kvh = bid & 7, b = bid >> 5, s = (bid >> 3) & 3;
  const int g = lane >> 4, c = lane & 15;
  const size_t rb = ((size_t)(b * 8 + kvh) * SK + s * 1024) * DH;
  const __amdgpu_buffer_rsrc_t rk = __builtin_amdgcn_make_buffer_rsrc((void*)(Kc + rb), 0, 1024 * 512, 0x00020000);
  const __amdgpu_buffer_rsrc_t rv = __builtin_amdgcn_make_buffer_rsrc((void*)(Vc + rb), 0, 1024 * 512, 0x00020000);
  const int voff = (4 * w + g) * 512 + c * 16;
  v4u qv[2][2];
#pragma unroll
  for (int h = 0; h < 2; ++h)
#pragma unroll
    for (int hf = 0; hf < 2; ++hf) qv[h][hf] = *(const v4u*)(q + b * 4096 + (kvh * 2 + h) * 256 + hf * 128 + c * 8);
  __builtin_amdgcn_sched_barrier(0);
  v4u a[RING];
#pragma unroll
  for (int i = 0; i < RING; ++i) a[i] = __builtin_amdgcn_raw_buffer_load_b128(rk, voff + (i & 1) * 256, (i >> 1) * 8192, 2);
  __builtin_amdgcn_sched_barrier(0);
  asm volatile("" : "+v"(qv[0][0]), "+v"(qv[0][1]), "+v"(qv[1][0]), "+v"(qv[1][1]));
  float sc[4][2];
  for (int n = 0; n < 4; ++n) {
    float x[16][2];
    const __amdgpu_buffer_rsrc_t rn = n < 3 ? rk : rv;
    const int so = n < 3 ? (n + 1) * 131072 : 0;
#pragma unroll
    for (int i = 0; i < RING; ++i) {
      const v4u cur = a[i];
#pragma unroll
      for (int h = 0; h < 2; ++h) {
        float d = (i & 1) ? x[i >> 1][h] : 0.f;
#pragma unroll
        for (int k = 0; k < 4; ++k) d = dot2(cur[k], qv[h][i & 1][k], d);
        x[i >> 1][h] = d;
      }
      __builtin_amdgcn_sched_barrier(0);
      a[i] = __builtin_amdgcn_raw_buffer_load_b128(rn, voff + (i & 1) * 256, so + (i >> 1) * 8192, 2);
      __builtin_amdgcn_sched_barrier(0);
    }
#define TR_STEP(HALF, CTRL, BIT) _Pragma("unroll") for (int u = 0; u < (HALF); ++u) _Pragma("unroll") for (int h = 0; h < 2; ++h) { \
      const float lo_ = x[u][h], hi_ = x[u + (HALF)][h]; const float t1_ = lo_ + dppf<CTRL>(lo_), t2_ = hi_ + dppf<CTRL>(hi_); x[u][h] = (c & (BIT)) ? t2_ : t1_; }
    TR_STEP(8, XOR8, 8) TR_STEP(4, XOR7, 4) TR_STEP(2, XOR2, 2) TR_STEP(1, XOR1, 1)
#undef TR_STEP
#pragma unroll
    for (int h = 0; h < 2; ++h) { sc[0][h] = sc[1][h]; sc[1][h] = sc[2][h]; sc[2][h] = sc[3][h]; sc[3][h] = rbf(x[0][h]); }
  }
  constexpr float C2 = 0.0625f * 1.4426950408889634f;
  float m[2], l[2]; unsigned pp[4];
#pragma unroll
  for (int h = 0; h < 2; ++h) m[h] = wave_max(fmaxf(fmaxf(sc[0][h], sc[1][h]), fmaxf(sc[2][h], sc[3][h])));
  float p[4][2];
#pragma unroll
  for (int h = 0; h < 2; ++h) { float t = 0.f;
#pragma unroll
    for (int n = 0; n < 4; ++n) { p[n][h] = __builtin_amdgcn_exp2f((sc[n][h] - m[h]) * C2); t += p[n][h]; }
    l[h] = wave_sum(t); }
#pragma unroll
  for (int n = 0; n < 4; ++n) pp[n] = cvtpk(p[n][0], p[n][1]);
  unsigned sel[4];
  {
    const int h = c >> 3, jp = c & 7;
    const unsigned b0 = 2 * h, b1 = 2 * h + 1;
    const unsigned lo = 0x0c0c0000u | (b1 << 8) | b0, hi = 0x00000c0cu | (b1 << 24) | (b0 << 16);
#pragma unroll
    for (int k = 0; k < 4; ++k) sel[k] = (k == (jp >> 1)) ? ((jp & 1) ? hi : lo) : 0x0c0c0c0cu;
  }
  f32x4 acc[2] = {{0.f, 0.f, 0.f, 0.f}, {0.f, 0.f, 0.f, 0.f}};
#define PV_BLOCK(REISSUE) \
  { const unsigned pc = pp[0]; pp[0] = pp[1]; pp[1] = pp[2]; pp[2] = pp[3]; \
    _Pragma("unroll") for (int u = 0; u < 16; ++u) { \
      unsigned pb; \
      switch (u) { default: pb = dppu<0x150>(pc); break; case 1: pb = dppu<0x151>(pc); break; case 2: pb = dppu<0x152>(pc); break; case 3: pb = dppu<0x153>(pc); break; \
        case 4: pb = dppu<0x154>(pc); break; case 5: pb = dppu<0x155>(pc); break; case 6: pb = dppu<0x156>(pc); break; case 7: pb = dppu<0x157>(pc); break; \
        case 8: pb = dppu<0x158>(pc); break; case 9: pb = dppu<0x159>(pc); break; case 10: pb = dppu<0x15a>(pc); break; case 11: pb = dppu<0x15b>(pc); break; \
        case 12: pb = dppu<0x15c>(pc); break; case 13: pb = dppu<0x15d>(pc); break; case 14: pb = dppu<0x15e>(pc); break; case 15: pb = dppu<0x15f>(pc); break; } \
      v4u af; \
      _Pragma("unroll") for (int k = 0; k < 4; ++k) af[k] = __builtin_amdgcn_perm(pb, pb, sel[k]); \
      _Pragma("unroll") for (int hf = 0; hf < 2; ++hf) { \
        const v4u cur = a[2 * u + hf]; \
        acc[hf] = mfma16(af, cur, acc[hf]); \
        __builtin_amdgcn_sched_barrier(0); \
        if (REISSUE) a[2 * u + hf] = __builtin_amdgcn_raw_buffer_load_b128(rv, voff + hf * 256, so + u * 8192, 2); \
        __builtin_amdgcn_sched_barrier(0); } } }
#pragma nounroll
  for (int n = 0; n < 3; ++n) { const int so = (n + 1) * 131072; PV_BLOCK(true) }
  { const int so = 0; (void)so; PV_BLOCK(false) }
  const int slot = (bid * 4 + w);
  if (lane == 0) { pml[slot * 4 + 0] = m[0]; pml[slot * 4 + 1] = l[0]; pml[slot * 4 + 2] = m[1]; pml[slot * 4 + 3] = l[1]; }
#pragma unroll
  for (int hf = 0; hf < 2; ++hf) *(f32x4*)(po + (size_t)slot * 512 + (g >> 1) * 256 + hf * 128 + 8 * c + 4 * (g & 1)) = acc[hf];
}

__global__ void __launch_bounds__(256) k_comb(const float* pml, const float* po, unsigned short* o) {
  const int b = blockIdx.x >> 4, h = blockIdx.x & 15, kvh = h >> 1, hh = h & 1, d = threadIdx.x;
  constexpr float C2 = 0.0625f * 1.4426950408889634f;
  float M = -INFINITY;
  for (int i = 0; i < 16; ++i) { int slot = ((b * 32 + (i >> 2) * 8 + kvh) * 4 + (i & 3)); M = fmaxf(M, pml[slot * 4 + hh * 2]); }
  float L = 0.f, acc = 0.f;
  for (int i = 0; i < 16; ++i) { int slot = ((b * 32 + (i >> 2) * 8 + kvh) * 4 + (i & 3));
    float wt = exp2f((pml[slot * 4 + hh * 2] - M) * C2); L += wt * pml[slot * 4 + hh * 2 + 1]; acc += wt * po[(size_t)slot * 512 + hh * 256 + d]; }
  o[b * 4096 + h * 256 + d] = f2bf(acc / L);
}

__global__ void __launch_bounds__(512) k_wo(const unsigned short* o, const unsigned short* wo, const unsigned short* x, float* h) {
  __shared__ float red[8][64];
  const int tid = threadIdx.x, n = tid & 7, b = (tid >> 3) & 7, kq = tid >> 6, col = blockIdx.x * 8 + n;
  float acc = 0.f;
  for (int k = kq * 512; k < kq * 512 + 512; ++k) acc += bf2f(o[b * 4096 + k]) * bf2f(wo[(size_t)k * 448 + col]);
  red[kq][tid & 63] = acc;
  __syncthreads();
  if (tid < 64) { float t = 0.f; for (int i = 0; i < 8; ++i) t += red[i][tid]; h[b * 448 + col] = rbf(bf2f(x[b * 448 + col]) + rbf(t)); }
}

__global__ void __launch_bounds__(512) k_up(const float* h, const unsigned short* w1, const unsigned short* b1, float* h1) {
  __shared__ float xn[8][448];
  __shared__ float red[4][128];
  const int tid = threadIdx.x, lane = tid & 63, w = tid >> 6;
  float v[7], o[7];
#pragma unroll
  for (int i = 0; i < 7; ++i) v[i] = h[w * 448 + lane + 64 * i];
  ln_row(v, o);
#pragma unroll
  for (int i = 0; i < 7; ++i) xn[w][lane + 64 * i] = o[i];
  __syncthreads();
  const int n = tid & 15, b = (tid >> 4) & 7, kq = tid >> 7;
  const int col = blockIdx.x * 16 + n;
  float acc = 0.f;
  for (int k = kq * 112; k < kq * 112 + 112; ++k) acc += xn[b][k] * bf2f(w1[(size_t)k * 1792 + col]);
  red[kq][tid & 127] = acc;
  __syncthreads();
  if (tid < 128) { float t = rbf(rbf(red[0][tid] + red[1][tid] + red[2][tid] + red[3][tid]) + bf2f(b1[col])); t = t > 0.f ? t : 0.f; h1[b * 1792 + col] = rbf(t * t); }
}

__global__ void __launch_bounds__(512) k_down(const float* h1, const unsigned short* w2, const unsigned short* b2, const float* h, unsigned short* out) {
  __shared__ float red[8][64];
  const int tid = threadIdx.x, n = tid & 7, b = (tid >> 3) & 7, kq = tid >> 6, col = blockIdx.x * 8 + n;
  float acc = 0.f;
  for (int k = kq * 224; k < kq * 224 + 224; ++k) acc += h1[b * 1792 + k] * bf2f(w2[(size_t)k * 448 + col]);
  red[kq][tid & 63] = acc;
  __syncthreads();
  if (tid < 64) { float t = 0.f; for (int i = 0; i < 8; ++i) t += red[i][tid]; out[b * 448 + col] = f2bf(h[b * 448 + col] + rbf(rbf(t) + bf2f(b2[col]))); }
}

extern "C" void kernel_launch(void* const* d_in, const int* in_sizes, int n_in, void* d_out, int out_size, void* d_ws, size_t ws_size, hipStream_t stream) {
  const unsigned short* x = (const unsigned short*)d_in[0];
  const unsigned short* Kc = (const unsigned short*)d_in[1];
  const unsigned short* Vc = (const unsigned short*)d_in[2];
  const unsigned short* wq = (const unsigned short*)d_in[3];
  const unsigned short* wo = (const unsigned short*)d_in[4];
  const unsigned short* w1 = (const unsigned short*)d_in[6];
  const unsigned short* b1 = (const unsigned short*)d_in[7];
  const unsigned short* w2 = (const unsigned short*)d_in[8];
  const unsigned short* b2 = (const unsigned short*)d_in[9];
  char* ws = (char*)d_ws;
  unsigned short* q = (unsigned short*)ws;
  unsigned short* o = (unsigned short*)(ws + 65536);
  float* pml = (float*)(ws + 131072);
  float* h = (float*)(ws + 196608);
  float* h1 = (float*)(ws + 262144);
  float* po = (float*)(ws + 1048576);
  k_qproj<<<256, 512, 0, stream>>>(x, wq, q);
  k_attn<<<256, 256, 0, stream>>>(Kc, Vc, q, pml, po);
  k_comb<<<128, 256, 0, stream>>>(pml, po, o);
  k_wo<<<56, 512, 0, stream>>>(o, wo, x, h);
  k_up<<<112, 512, 0, stream>>>(h, w1, b1, h1);
  k_down<<<56, 512, 0, stream>>>(h1, w2, b2, h, (unsigned short*)d_out);
}
```

```cpp
#include <hip/hip_runtime.h>
#include <hip/hip_bf16.h>
#include <stdint.h>

typedef unsigned v4u __attribute__((ext_vector_type(4)));
typedef unsigned v2u __attribute__((ext_vector_type(2)));
typedef float f32x4 __attribute__((ext_vector_type(4)));
typedef float f32x2 __attribute__((ext_vector_type(2)));
typedef short bf16x8 __attribute__((ext_vector_type(8)));
typedef short s16x4 __attribute__((ext_vector_type(4)));
typedef __bf16 bf16x2 __attribute__((ext_vector_type(2)));
typedef unsigned short u16;
typedef unsigned long long u64;
#define LAS __attribute__((address_space(3)))
#define GAS __attribute__((address_space(1)))

constexpr int SK = 4096, DH = 256;
constexpr int NT = 2, SC1 = 16;
constexpr unsigned SENT = 0xFFFFFFFFu;
constexpr unsigned SPIN_CAP = 1u << 18;

#define PRE_F 4
#define FILL_F 6
#define F_LO 4
#define F_HI 8
#define PACE_N 8
#define W_INFL 3
#define TAU16 226
#define T0_16 1650
#define CK_T 2500
#define CK_OFF 28
#define CK_BIAS 300
#define QD 80
#define TS1 186
#define TS2 270
#define D1 35
#define D2 10
#define D3 5

constexpr int WS_E = 0;
constexpr int WS_RDY = 4096;
constexpr int WS_ML = 8192;
constexpr int WS_CK = 12288;
constexpr int WS_P2 = 32768;
constexpr int WS_O1 = 131072;
constexpr int WS_QG = WS_O1 + 262144;
constexpr int WS_P4 = WS_QG + 524288;
constexpr int P4S = 64;
constexpr int WS_END = WS_P4 + 8 * 28 * 32 * P4S;

constexpr int L_WA = 0;
constexpr int L_WB = 65536;
constexpr int OI_STRIDE = 1056;
constexpr int L_OI = L_WB;
constexpr int L_PA = L_WB + 16384;
constexpr int L_WQ = L_WB + 57344;
constexpr int XN_STRIDE = 912;
constexpr int L_XN = L_WQ + 14336;
constexpr int L_MRG = L_XN + 8 * XN_STRIDE;
constexpr int L_Q = L_MRG + 8192;
constexpr int L_H1 = L_Q + 1024;
constexpr int L_ML = L_H1 + 256;
constexpr int L_CF = L_ML + 64;
constexpr int L_FL = L_CF + 32;
constexpr int L_END = L_FL + 32;
static_assert(L_END <= 163840 && (L_MRG % 16) == 0 && (L_ML % 16) == 0, "lds map");

__device__ __forceinline__ float bf2f(unsigned u) { return __builtin_bit_cast(float, u << 16); }
__device__ __forceinline__ float bfhi(unsigned u) { return __builtin_bit_cast(float, u & 0xffff0000u); }
__device__ __forceinline__ unsigned cvtpk(float lo, float hi) { f32x2 v = {lo, hi}; return __builtin_bit_cast(unsigned, __builtin_convertvector(v, bf16x2)); }
__device__ __forceinline__ float rbf(float x) { return bf2f(cvtpk(x, x)); }
__device__ __forceinline__ float dot2(unsigned a, unsigned b, float c) {
  return __builtin_amdgcn_fdot2_f32_bf16(__builtin_bit_cast(bf16x2, a), __builtin_bit_cast(bf16x2, b), c, false);
}
template <int CTRL> __device__ __forceinline__ float dppf(float x) {
  return __builtin_bit_cast(float, __builtin_amdgcn_mov_dpp(__builtin_bit_cast(int, x), CTRL, 0xf, 0xf, true));
}
template <int CTRL> __device__ __forceinline__ unsigned dppu(unsigned x) { return (unsigned)__builtin_amdgcn_mov_dpp((int)x, CTRL, 0xf, 0xf, true); }
constexpr int XOR1 = 0xB1, XOR2 = 0x4E, XOR7 = 0x141, XOR8 = 0x128, ROR4 = 0x124, BCAST = 0x150;
__device__ __forceinline__ float sw16_sum(float a, float b) {
  auto s = __builtin_amdgcn_permlane16_swap(__float_as_uint(a), __float_as_uint(b), false, false);
  return __uint_as_float(s[0]) + __uint_as_float(s[1]);
}
__device__ __forceinline__ float sw32_sum(float a, float b) {
  auto s = __builtin_amdgcn_permlane32_swap(__float_as_uint(a), __float_as_uint(b), false, false);
  return __uint_as_float(s[0]) + __uint_as_float(s[1]);
}
__device__ __forceinline__ float sw16_max(float a) {
  auto s = __builtin_amdgcn_permlane16_swap(__float_as_uint(a), __float_as_uint(a), false, false);
  return fmaxf(__uint_as_float(s[0]), __uint_as_float(s[1]));
}
__device__ __forceinline__ float sw32_max(float a) {
  auto s = __builtin_amdgcn_permlane32_swap(__float_as_uint(a), __float_as_uint(a), false, false);
  return fmaxf(__uint_as_float(s[0]), __uint_as_float(s[1]));
}
__device__ __forceinline__ float row_sum(float x) { x += dppf<XOR1>(x); x += dppf<XOR2>(x); x += dppf<XOR7>(x); x += dppf<XOR8>(x); return x; }
__device__ __forceinline__ float wave_max(float x) {
  x = fmaxf(x, dppf<XOR1>(x)); x = fmaxf(x, dppf<XOR2>(x)); x = fmaxf(x, dppf<XOR7>(x)); x = fmaxf(x, dppf<XOR8>(x));
  return sw32_max(sw16_max(x));
}
__device__ __forceinline__ float half_sum(float x) { x = row_sum(x); return sw16_sum(x, x); }
__device__ __forceinline__ float wave_sum(float x) { x = half_sum(x); return sw32_sum(x, x); }
__device__ __forceinline__ f32x4 mfma16(v4u a, v4u b, f32x4 c) {
  return __builtin_amdgcn_mfma_f32_16x16x32_bf16(__builtin_bit_cast(bf16x8, a), __builtin_bit_cast(bf16x8, b), c, 0, 0, 0);
}
__device__ __forceinline__ v2u tr_read(LAS unsigned char* p) {
  return __builtin_bit_cast(v2u, __builtin_amdgcn_ds_read_tr16_b64_v4i16((LAS s16x4*)p));
}
__device__ __forceinline__ v4u tr_frag(LAS unsigned char* p, int stride) {
  const v2u lo = tr_read(p), hi = tr_read(p + 4 * stride);
  return v4u{lo.x, lo.y, hi.x, hi.y};
}
__device__ __forceinline__ void dmaw(const void* src, LAS unsigned char* dst) {
  __builtin_amdgcn_global_load_lds((const GAS unsigned*)src, (LAS unsigned*)dst, 16, 0, 0);
  asm volatile("s_waitcnt vmcnt(%0)" :: "i"(W_INFL - 1) : "memory");
}
__device__ __forceinline__ unsigned now() { return (unsigned)__builtin_amdgcn_s_memrealtime(); }
__device__ __forceinline__ unsigned nosent(unsigned u) { return u < 0xFFFFFFFEu ? u : 0xFFFFFFFEu; }
__device__ __forceinline__ unsigned umax3(unsigned a, unsigned b, unsigned c) { unsigned t = a > b ? a : b; return t > c ? t : c; }
__device__ __forceinline__ unsigned umax4(v4u v, unsigned m) { return umax3(umax3(m, v.x, v.y), v.z, v.w); }
#define VM0() do { asm volatile("s_waitcnt vmcnt(0)" ::: "memory"); __builtin_amdgcn_s_waitcnt(0x0F70); } while (0)
#define LGKM0() asm volatile("s_waitcnt lgkmcnt(0)" ::: "memory")
#define BARRIER() do { asm volatile("s_waitcnt lgkmcnt(0)" ::: "memory"); __builtin_amdgcn_s_barrier(); asm volatile("" ::: "memory"); } while (0)
#define MEMFENCE() asm volatile("" ::: "memory")
#define ARRIVE(i) do { if (lane == 0) __hip_atomic_fetch_add((LAS unsigned*)(L + L_FL + 4 * (i)), 1u, __ATOMIC_RELAXED, __HIP_MEMORY_SCOPE_WORKGROUP); } while (0)
__device__ __forceinline__ void hold(int D, unsigned T) {
  const unsigned n = now() + (unsigned)D;
  unsigned e = (int)(n - T) > 0 ? n : T;
  e = (int)(e - n) > 120 ? n + 120u : e;
  for (unsigned sp = 0; (int)(now() - e) < 0 && sp < SPIN_CAP; ++sp) {}
}

constexpr int RING = 32;
constexpr float C2 = 0.0625f * 1.4426950408889634f;

__global__ void __launch_bounds__(512, 2)
fused_decode_block(unsigned char* ws, const u16* __restrict__ x, const u16* __restrict__ wq, const u16* __restrict__ Kc, const u16* __restrict__ Vc,
                   const u16* __restrict__ wo, const u16* __restrict__ w1, const u16* __restrict__ w2, const u16* __restrict__ b1, const u16* __restrict__ b2,
                   u16* __restrict__ out) {
  __shared__ __attribute__((aligned(16))) unsigned char smem[L_END];
  LAS unsigned char* L = (LAS unsigned char*)smem;
#define KARGS() asm volatile("" :: "s"(ws), "s"(x), "s"(wq), "s"(Kc), "s"(Vc), "s"(wo), "s"(w1), "s"(w2), "s"(b1), "s"(b2), "s"(out)); \
  const __amdgpu_buffer_rsrc_t rw = __builtin_amdgcn_make_buffer_rsrc((void*)ws, 0, WS_END, 0x00020000);
  const int tid = threadIdx.x, lane = tid & 63, wv = __builtin_amdgcn_readfirstlane(tid >> 6);
  const int bid = blockIdx.x, kvh = bid & 7, s = (bid >> 3) & 3, b = bid >> 5;
  const int g = lane >> 4, c = lane & 15;
  volatile LAS unsigned* flags = (volatile LAS unsigned*)(L + L_FL);

  if (wv >= 4) {
    const int w = wv - 4;
    KARGS()
    __builtin_amdgcn_s_waitcnt(0);
    const size_t rb = ((size_t)(b * 8 + kvh) * SK + s * 1024) * DH;
    const __amdgpu_buffer_rsrc_t rk = __builtin_amdgcn_make_buffer_rsrc((void*)(Kc + rb), 0, 1024 * 512, 0x00020000);
    const __amdgpu_buffer_rsrc_t rv = __builtin_amdgcn_make_buffer_rsrc((void*)(Vc + rb), 0, 1024 * 512, 0x00020000);
    const int voff = (4 * w + g) * 512 + c * 16;
    v4u a[RING];
    unsigned dl = (now() << 4) + T0_16 + (32 - PACE_N) * TAU16;
    int slack = 0;
#define THROTTLE() asm volatile("s_waitcnt vmcnt(%1)\n\ts_cmp_lt_i32 %0, 0\n\ts_cbranch_scc1 1f\n\ts_waitcnt vmcnt(%2)\n1:" :: "s"(slack), "i"(F_HI - 2), "i"(F_LO - 2) : "scc")
#define PACE() do { dl += PACE_N * TAU16; slack = (int)(dl - (now() << 4)) + __builtin_amdgcn_readfirstlane((int)flags[4]); \
                    *(volatile LAS int*)(L + L_XN + w * 256 + lane * 4) = slack; } while (0)
#pragma unroll
    for (int i = 0; i < PRE_F; ++i) a[i] = __builtin_amdgcn_raw_buffer_load_b128(rk, voff + (i & 1) * 256, (i >> 1) * 8192, NT);
    __builtin_amdgcn_sched_barrier(0);
    __builtin_amdgcn_s_barrier();
#pragma unroll
    for (int i = PRE_F; i < RING; ++i) {
      if (i >= FILL_F && (i & 1) == 0) asm volatile("s_waitcnt vmcnt(%0)" :: "i"(FILL_F - 2));
      a[i] = __builtin_amdgcn_raw_buffer_load_b128(rk, voff + (i & 1) * 256, (i >> 1) * 8192, NT);
      __builtin_amdgcn_sched_barrier(0);
    }
    for (unsigned sp = 0; flags[1] < 4u && sp < SPIN_CAP * 16u; ++sp) __builtin_amdgcn_s_sleep(1);
    MEMFENCE();
    PACE();
    v4u qv[2][2];
#pragma unroll
    for (int h = 0; h < 2; ++h)
#pragma unroll
      for (int hf = 0; hf < 2; ++hf) qv[h][hf] = *(LAS v4u*)(L + L_Q + (h * 256 + hf * 128 + c * 8) * 2);
    float sc[4][2];
#define TR_STEP(HALF, CTRL, BIT) _Pragma("unroll") for (int u = 0; u < (HALF); ++u) _Pragma("unroll") for (int h = 0; h < 2; ++h) { \
      const float lo_ = xs[u][h], hi_ = xs[u + (HALF)][h]; const float t1_ = lo_ + dppf<CTRL>(lo_), t2_ = hi_ + dppf<CTRL>(hi_); xs[u][h] = (c & (BIT)) ? t2_ : t1_; }
#pragma nounroll
    for (int n = 0; n < 4; ++n) {
      const __amdgpu_buffer_rsrc_t rn = n < 3 ? rk : rv;
      const int so = n < 3 ? (n + 1) * 131072 : 0;
      float xs[16][2];
#pragma unroll
      for (int i = 0; i < RING; ++i) {
        const v4u cur = a[i];
#pragma unroll
        for (int h = 0; h < 2; ++h) {
          float d = (i & 1) ? xs[i >> 1][h] : 0.f;
#pragma unroll
          for (int k = 0; k < 4; ++k) d = dot2(cur[k], qv[h][i & 1][k], d);
          xs[i >> 1][h] = d;
        }
        __builtin_amdgcn_sched_barrier(0);
        if ((i & 1) == 0) THROTTLE();
        a[i] = __builtin_amdgcn_raw_buffer_load_b128(rn, voff + (i & 1) * 256, so + (i >> 1) * 8192, NT);
        if ((i & (PACE_N - 1)) == PACE_N - 1) PACE();
        __builtin_amdgcn_sched_barrier(0);
      }
      TR_STEP(8, XOR8, 8) TR_STEP(4, XOR7, 4) TR_STEP(2, XOR2, 2) TR_STEP(1, XOR1, 1)
#pragma unroll
      for (int h = 0; h < 2; ++h) { sc[0][h] = sc[1][h]; sc[1][h] = sc[2][h]; sc[2][h] = sc[3][h]; sc[3][h] = rbf(xs[0][h]); }
    }
    float m[2], l[2], p[4][2]; unsigned pp[4];
#pragma unroll
    for (int h = 0; h < 2; ++h) m[h] = wave_max(fmaxf(fmaxf(sc[0][h], sc[1][h]), fmaxf(sc[2][h], sc[3][h])));
#pragma unroll
    for (int h = 0; h < 2; ++h) { float t = 0.f;
#pragma unroll
      for (int n = 0; n < 4; ++n) { p[n][h] = __builtin_amdgcn_exp2f((sc[n][h] - m[h]) * C2); t += p[n][h]; }
      l[h] = wave_sum(t); }
#pragma unroll
    for (int n = 0; n < 4; ++n) pp[n] = cvtpk(p[n][0], p[n][1]);
    if (lane == 0) *(LAS v4u*)(L + L_ML + w * 16) = v4u{nosent(__float_as_uint(m[0])), nosent(__float_as_uint(l[0])), nosent(__float_as_uint(m[1])), nosent(__float_as_uint(l[1]))};
    unsigned sel[4];
    {
      const unsigned h = c >> 3, jp = c & 7, b0 = 2 * h, b1_ = 2 * h + 1;
      const unsigned lo = 0x0c0c0000u | (b1_ << 8) | b0, hi = 0x00000c0cu | (b1_ << 24) | (b0 << 16);
#pragma unroll
      for (int k = 0; k < 4; ++k) sel[k] = ((unsigned)k == (jp >> 1)) ? ((jp & 1) ? hi : lo) : 0x0c0c0c0cu;
    }
    f32x4 acc[2] = {{0.f, 0.f, 0.f, 0.f}, {0.f, 0.f, 0.f, 0.f}};
#define PV_STEP(U, REISSUE) { const unsigned pb = dppu<BCAST + (U)>(pc); v4u af; \
      _Pragma("unroll") for (int k = 0; k < 4; ++k) af[k] = __builtin_amdgcn_perm(pb, pb, sel[k]); \
      _Pragma("unroll") for (int hf = 0; hf < 2; ++hf) { \
        acc[hf] = mfma16(af, a[2 * (U) + hf], acc[hf]); \
        __builtin_amdgcn_sched_barrier(0); \
        if (REISSUE && hf == 0) THROTTLE(); \
        if (REISSUE) a[2 * (U) + hf] = __builtin_amdgcn_raw_buffer_load_b128(rv, voff + hf * 256, so + (U) * 8192, NT); \
        if (REISSUE && ((U) & (PACE_N / 2 - 1)) == PACE_N / 2 - 1 && hf == 1) PACE(); \
        __builtin_amdgcn_sched_barrier(0); } }
#define PV_BLOCK(R) { const unsigned pc = pp[0]; pp[0] = pp[1]; pp[1] = pp[2]; pp[2] = pp[3]; \
      PV_STEP(0, R) PV_STEP(1, R) PV_STEP(2, R) PV_STEP(3, R) PV_STEP(4, R) PV_STEP(5, R) PV_STEP(6, R) PV_STEP(7, R) \
      PV_STEP(8, R) PV_STEP(9, R) PV_STEP(10, R) PV_STEP(11, R) PV_STEP(12, R) PV_STEP(13, R) PV_STEP(14, R) PV_STEP(15, R) }
#pragma nounroll
    for (int n = 0; n < 3; ++n) { const int so = (n + 1) * 131072; PV_BLOCK(true) }
    unsigned cfu;
    { unsigned sp = 0; do { cfu = *(volatile LAS unsigned*)(L + L_CF + w * 8 + (g >> 1) * 4); } while (__any(cfu == SENT) && ++sp < SPIN_CAP * 16u); }
    unsigned mw_ = (unsigned)(size_t)L + L_MRG + w * 2048 + ((g >> 1) * 256 + 8 * c + 4 * (g & 1)) * 4, mr_ = (unsigned)(size_t)L + L_MRG + lane * 32;
    int o1st = WS_O1 + ((b * 8 + kvh) * 4 + s) * 1024 + lane * 16;
    asm volatile("" : "+v"(mw_), "+v"(mr_), "+v"(o1st));
    { const int so = 0; (void)so; PV_BLOCK(false) }
    const float cf = __uint_as_float(cfu);
#pragma unroll
    for (int hf = 0; hf < 2; ++hf) *(LAS f32x4*)(size_t)(mw_ + hf * 512) = acc[hf] * cf;
    BARRIER();
    if (w == 0) {
      __builtin_amdgcn_s_setprio(3);
      f32x4 lo[4], hi[4];
#pragma unroll
      for (int u = 0; u < 4; ++u) { lo[u] = *(LAS f32x4*)(size_t)(mr_ + u * 2048); hi[u] = *(LAS f32x4*)(size_t)(mr_ + u * 2048 + 16); }
      const f32x4 sl = (lo[0] + lo[1]) + (lo[2] + lo[3]), sh = (hi[0] + hi[1]) + (hi[2] + hi[3]);
      const v4u o = {nosent(cvtpk(sl[0], sl[1])), nosent(cvtpk(sl[2], sl[3])), nosent(cvtpk(sh[0], sh[1])), nosent(cvtpk(sh[2], sh[3]))};
      __builtin_amdgcn_raw_buffer_store_b128(o, rw, o1st, 0, SC1);
    }
    __builtin_amdgcn_endpgm();
  }

  const int hw = wv;
  KARGS()
  const unsigned tstart = now();
  const int u2 = (b & 1) * 4 + s;
  const int j3 = s * 8 + kvh;
  const int kh = s & 1, ns = b * 2 + (s >> 1);
  const int xr = 2 * hw + (lane >> 5), xi = (lane & 31) < 28 ? (lane & 31) : 27;
  v2u eraw = __builtin_amdgcn_raw_buffer_load_b64(rw, WS_E, 0, SC1);
  __builtin_amdgcn_sched_barrier(0);
  v4u xa = *(const GAS v4u*)(x + xr * 448 + xi * 16), xb = *(const GAS v4u*)(x + xr * 448 + xi * 16 + 8);
  v4u wqr[4];
  {
    const __amdgpu_buffer_rsrc_t rq = __builtin_amdgcn_make_buffer_rsrc((void*)wq, 0, 448 * 4096 * 2, 0x00020000);
    const int off = ((kh * 224 + hw * 16 + (lane >> 2)) * 4096 + kvh * 512 + ns * 32 + (lane & 3) * 8) * 2;
#pragma unroll
    for (int i = 0; i < 4; ++i) wqr[i] = __builtin_amdgcn_raw_buffer_load_b128(rq, off, (i < 3 || hw < 2 ? i : 0) * 64 * 8192, 0);
  }
  if (hw == 0 && lane < 32) *(LAS unsigned*)(L + L_ML + lane * 4) = lane < 24 ? SENT : 0u;
  LGKM0();
  __builtin_amdgcn_s_barrier();
  asm volatile("" : "+v"(xa), "+v"(xb), "+v"(eraw));
  const u64 tag = __builtin_bit_cast(u64, eraw) + 1ull;
  {
    float v[16];
#pragma unroll
    for (int k = 0; k < 4; ++k) { v[2 * k] = bf2f(xa[k]); v[2 * k + 1] = bfhi(xa[k]); v[8 + 2 * k] = bf2f(xb[k]); v[9 + 2 * k] = bfhi(xb[k]); }
    const bool live = (lane & 31) < 28;
    float sm = 0.f;
#pragma unroll
    for (int k = 0; k < 4; ++k) { sm = dot2(xa[k], 0x3f803f80u, sm); sm = dot2(xb[k], 0x3f803f80u, sm); }
    const float mean = half_sum(live ? sm : 0.f) * (1.f / 448.f);
    float sq = 0.f;
#pragma unroll
    for (int k = 0; k < 16; ++k) { const float d = v[k] - mean; sq = fmaf(d, d, sq); }
    const float var = rbf(half_sum(live ? sq : 0.f) * (1.f / 448.f));
    const float mb = rbf(mean), rden = __builtin_amdgcn_rcpf(rbf(__builtin_amdgcn_sqrtf(rbf(var + 1e-5f))));
    v4u o0, o1;
#pragma unroll
    for (int k = 0; k < 4; ++k) {
      const unsigned da = cvtpk(v[2 * k] - mb, v[2 * k + 1] - mb), db = cvtpk(v[8 + 2 * k] - mb, v[9 + 2 * k] - mb);
      o0[k] = cvtpk(bf2f(da) * rden, bfhi(da) * rden); o1[k] = cvtpk(bf2f(db) * rden, bfhi(db) * rden);
    }
    if (live) { *(LAS v4u*)(L + L_XN + xr * XN_STRIDE + xi * 32) = o0; *(LAS v4u*)(L + L_XN + xr * XN_STRIDE + xi * 32 + 16) = o1; }
  }
  __builtin_amdgcn_sched_barrier(0);
#pragma unroll
  for (int i = 0; i < 4; ++i) if (i < 3 || hw < 2) *(LAS v4u*)(L + L_WQ + (hw + 4 * i) * 1024 + lane * 16) = wqr[i];
  LGKM0();
  ARRIVE(0);
  const unsigned tl = (unsigned)tag << 28, th = (unsigned)(tag >> 4);
  for (unsigned sp = 0; flags[0] < 4u && sp < SPIN_CAP * 16u; ++sp) {}
  MEMFENCE();
  if (hw < 2) {
    LAS unsigned char* wp = L + L_WQ + (8 * g + (c >> 2)) * 64 + hw * 32 + (c & 3) * 8;
    LAS unsigned char* xp = L + L_XN + (c & 7) * XN_STRIDE + kh * 448 + g * 16;
    v4u fa[7], fb[7];
#pragma unroll
    for (int t = 0; t < 7; ++t) { fa[t] = tr_frag(wp + t * 2048, 64); fb[t] = *(LAS v4u*)(xp + t * 64); }
    __builtin_amdgcn_sched_barrier(0);
    f32x4 d0 = {0.f, 0.f, 0.f, 0.f}, d1 = {0.f, 0.f, 0.f, 0.f};
#pragma unroll
    for (int t = 0; t < 7; ++t) { if (t & 1) d1 = mfma16(fa[t], fb[t], d1); else d0 = mfma16(fa[t], fb[t], d0); }
    d0 += d1;
    const unsigned ua = __float_as_uint(c < 8 ? d0[0] : d0[2]), ub = __float_as_uint(c < 8 ? d0[1] : d0[3]);
    const int off = WS_QG + ((((c & 7) * 8 + kvh) * 2 + kh) * 512 + ns * 32 + hw * 16 + 4 * g + (c >> 3) * 2) * 8;
    __builtin_amdgcn_raw_buffer_store_b128(v4u{tl | ((ua + 8u) >> 4), th, tl | ((ub + 8u) >> 4), th}, rw, off, 0, SC1);
  }
  hold(hw < 2 ? QD : QD + 10, tstart);
  {
    const int off = WS_QG + ((b * 8 + kvh) * 1024 + hw * 128 + lane * 2) * 8;
    v4u qa, qb;
#pragma nounroll
    for (unsigned sp = 0; sp < SPIN_CAP; ++sp) {
      MEMFENCE();
      qa = __builtin_amdgcn_raw_buffer_load_b128(rw, off, 0, SC1);
      qb = __builtin_amdgcn_raw_buffer_load_b128(rw, off + 4096, 0, SC1);
      const unsigned bad = (((qa.x ^ tl) | (qa.z ^ tl) | (qb.x ^ tl) | (qb.z ^ tl)) & 0xf0000000u) | (qa.y ^ th) | (qa.w ^ th) | (qb.y ^ th) | (qb.w ^ th);
      if (!__any(bad != 0u)) break;
    }
    *(LAS unsigned*)(L + L_Q + hw * 256 + lane * 4) = cvtpk(__uint_as_float(qa.x << 4) + __uint_as_float(qb.x << 4), __uint_as_float(qa.z << 4) + __uint_as_float(qb.z << 4));
    LGKM0();
    ARRIVE(1);
    for (unsigned sp = 0; flags[1] < 4u && sp < SPIN_CAP * 16u; ++sp) __builtin_amdgcn_s_sleep(2);
    MEMFENCE();
  }
  if (hw == 1) {
    const v4u sv = {SENT, SENT, SENT, SENT};
    __builtin_amdgcn_raw_buffer_store_b128(sv, rw, WS_O1 + ((b * 8 + kvh) * 4 + s) * 1024 + lane * 16, 0, SC1);
    if (lane < 16 && u2 < 7) __builtin_amdgcn_raw_buffer_store_b128(sv, rw, WS_P2 + ((((b & 6) + (lane >> 3)) * 8 + kvh) * 448 + 64 * u2) * 2 + (lane & 7) * 16, 0, SC1);
    if (lane == 32) __builtin_amdgcn_raw_buffer_store_b128(sv, rw, WS_ML + ((b * 8 + kvh) * 4 + s) * 16, 0, SC1);
    if (j3 < 28) {
      __builtin_amdgcn_raw_buffer_store_b128(sv, rw, WS_P4 + ((b * 28 + (lane >> 2)) * 32 + j3) * P4S + (lane & 3) * 16, 0, SC1);
      if (lane < 48) __builtin_amdgcn_raw_buffer_store_b128(sv, rw, WS_P4 + ((b * 28 + 16 + (lane >> 2)) * 32 + j3) * P4S + (lane & 3) * 16, 0, SC1);
    }
    VM0();
    if (lane == 0) __builtin_amdgcn_raw_buffer_store_b64(__builtin_bit_cast(v2u, (tag << 16) | (u64)(tstart & 0xffffu)), rw, WS_RDY + (b * 32 + kvh * 4 + s) * 8, 0, SC1);
  }
  int cA = 0, cst = hw == 1 ? 0 : 2; unsigned tb1 = tstart;
  auto ctrl = [&]() __attribute__((always_inline)) {
    if (cst < 2 && (int)(now() - tb1) >= CK_T + cst * 150) {
      if (cst == 0) {
        volatile LAS int* sq = (volatile LAS int*)(L + L_XN);
        const int sl = (sq[0] + sq[64] + sq[128] + sq[192]) >> 2;
        if (lane == 0) __builtin_amdgcn_raw_buffer_store_b64(v2u{(unsigned)sl, (unsigned)tag}, rw, WS_CK + (b * 32 + kvh * 4 + s) * 8, 0, SC1);
      } else {
        MEMFENCE();
        const v2u e = __builtin_amdgcn_raw_buffer_load_b64(rw, WS_CK + ((b & 6) * 32 + lane) * 8, 0, SC1);
        const bool ok = e.y == (unsigned)tag;
        const float sm = wave_sum(ok ? fminf(fmaxf((float)(int)e.x, -6000.f), 6000.f) : 0.f), cn = wave_sum(ok ? 1.f : 0.f);
        cA += (__builtin_amdgcn_readfirstlane((int)fmaxf(-sm / fmaxf(cn, 1.f) - (float)CK_BIAS, 0.f)) * CK_OFF) >> 4;
        if (lane == 0) flags[4] = (unsigned)cA;
      }
      ++cst;
    }
  };
  const int j3c = j3 < 28 ? j3 : 27;
  if (u2 < 7) {
    const u16* src = wo + (size_t)(kvh * 512 + hw * 128 + (lane >> 3)) * 448 + 64 * u2 + (lane & 7) * 8;
#pragma unroll 4
    for (int i = 0; i < 16; ++i) dmaw(src + (size_t)i * 8 * 448, L + L_WA + hw * 16384 + i * 1024);
  }
  if (hw == 1) {
    u64 f;
#pragma nounroll
    for (unsigned sp = 0; sp < SPIN_CAP; ++sp) {
      MEMFENCE();
      f = __builtin_bit_cast(u64, __builtin_amdgcn_raw_buffer_load_b64(rw, WS_RDY + ((b & 6) * 32 + lane) * 8, 0, SC1));
      if (__all((f >> 16) == (tag & 0xffffffffffffull))) break;
      __builtin_amdgcn_s_sleep(16);
    }
    MEMFENCE();
    const float dt = -wave_max((float)(short)(tstart - (unsigned)f));
    cA = __builtin_amdgcn_readfirstlane((int)fminf(fmaxf(dt, -150.f), 0.f));
    tb1 = tstart + cA; cA <<= 4;
    if (lane == 0) flags[4] = (unsigned)cA;
  }
  if (j3 < 28) {
    const u16* src = w1 + (size_t)(hw * 112 + (lane >> 3)) * 1792 + 64 * j3 + (lane & 7) * 8;
#pragma unroll 2
    for (int i = 0; i < 14; ++i) dmaw(src + (size_t)i * 8 * 1792, L + L_WB + hw * 14336 + i * 1024);
  }
  const float b1v = bf2f(b1[64 * j3c + 16 * hw + c]);
  const v4u xrow = *(const GAS v4u*)(x + b * 448 + 8 * (lane < 56 ? lane : 55));
  const int ocol = 16 * j3c + 4 * (c & 3) + g;
  const float b2v = bf2f(b2[ocol]);
  VM0();
  ARRIVE(2);
  for (unsigned sp = 0; flags[2] < 4u && sp < SPIN_CAP * 16u; ++sp) __builtin_amdgcn_s_sleep(2);
  MEMFENCE();
  v4u wf[16], f1[14];
  {
    LAS unsigned char* pa = L + L_WA + (8 * g + (c >> 2)) * 128 + hw * 32 + (c & 3) * 8;
#pragma unroll
    for (int t = 0; t < 16; ++t) wf[t] = tr_frag(pa + t * 4096, 128);
#pragma unroll
    for (int t = 0; t < 14; ++t) f1[t] = tr_frag(pa + L_WB + t * 4096, 128);
  }
  LGKM0();
#pragma unroll
  for (int t = 0; t < 16; ++t) asm volatile("" : "+v"(wf[t]));
#pragma unroll
  for (int t = 0; t < 14; ++t) asm volatile("" : "+v"(f1[t]));
  float csum;
  {
    f32x4 t = {0.f, 0.f, 0.f, 0.f};
    const v4u ones = {0x3f803f80u, 0x3f803f80u, 0x3f803f80u, 0x3f803f80u};
#pragma unroll
    for (int t_ = 0; t_ < 14; ++t_) t = mfma16(ones, f1[t_], t);
    csum = t[0];
  }
  ARRIVE(3);
  for (unsigned sp = 0; flags[3] < 4u && sp < SPIN_CAP * 16u; ++sp) __builtin_amdgcn_s_sleep(2);
  MEMFENCE();
  if (lane < 56) {
    *(LAS v4u*)(L + L_PA + lane * 256 + ((8 + 2 * hw) ^ (lane & 7)) * 16) = hw == 0 ? xrow : v4u{0u, 0u, 0u, 0u};
    *(LAS v4u*)(L + L_PA + lane * 256 + ((9 + 2 * hw) ^ (lane & 7)) * 16) = hw == 0 ? v4u{0x3f803f80u, 0x3f803f80u, 0x3f803f80u, 0x3f803f80u} : v4u{0u, 0u, 0u, 0u};
  }
  if (j3 < 28) {
    const u16* src = w2 + (size_t)64 * j3 * 448 + hw * 7168 + lane * 8;
#pragma nounroll
    for (int i = 0; i < 14; ++i) { dmaw(src + i * 512, L + L_WA + hw * 14336 + i * 1024); ctrl(); }
  }
  VM0();
  if (hw == 1) {
    const int h = lane & 1, u = (lane >> 1) & 3;
    v2u ml;
#pragma nounroll
    for (unsigned sp = 0; sp < SPIN_CAP * 16u; ++sp) {
      ml.x = *(volatile LAS unsigned*)(L + L_ML + u * 16 + h * 8); ml.y = *(volatile LAS unsigned*)(L + L_ML + u * 16 + h * 8 + 4);
      if (!__any(ml.x == SENT || ml.y == SENT)) break;
      __builtin_amdgcn_s_sleep(8); ctrl();
    }
    const float mw = __uint_as_float(ml.x), lw = __uint_as_float(ml.y);
    float ms = fmaxf(mw, dppf<XOR2>(mw)); ms = fmaxf(ms, dppf<ROR4>(ms));
    float ls = lw * __builtin_amdgcn_exp2f((mw - ms) * C2); ls += dppf<XOR2>(ls); ls += dppf<ROR4>(ls);
    if (lane < 2) __builtin_amdgcn_raw_buffer_store_b64(v2u{nosent(__float_as_uint(ms)), nosent(__float_as_uint(ls))}, rw, WS_ML + ((b * 8 + kvh) * 4 + s) * 16 + h * 8, 0, SC1);
#pragma nounroll
    for (unsigned sp = 0; cst < 2 && sp < SPIN_CAP; ++sp) { __builtin_amdgcn_s_sleep(8); ctrl(); }
    __builtin_amdgcn_s_sleep(40);
    v2u gl;
#pragma nounroll
    for (unsigned sp = 0; sp < SPIN_CAP; ++sp) {
      MEMFENCE();
      gl = __builtin_amdgcn_raw_buffer_load_b64(rw, WS_ML + ((b * 8 + kvh) * 4 + u) * 16 + h * 8, 0, SC1);
      if (!__any(gl.x == SENT || gl.y == SENT)) break;
      __builtin_amdgcn_s_sleep(32);
    }
    const float mg = __uint_as_float(gl.x), lg = __uint_as_float(gl.y);
    float M = fmaxf(mg, dppf<XOR2>(mg)); M = fmaxf(M, dppf<ROR4>(M));
    float Ls = lg * __builtin_amdgcn_exp2f((mg - M) * C2); Ls += dppf<XOR2>(Ls); Ls += dppf<ROR4>(Ls);
    const float cf = __builtin_amdgcn_exp2f((mw - M) * C2) / Ls;
    if (lane < 8) *(LAS unsigned*)(L + L_CF + lane * 4) = nosent(__float_as_uint(cf));
  }
#define PIN(v) asm volatile("" : "+v"(v))
#define LP(T, a) ((LAS T*)(size_t)(a))
  const unsigned lb = (unsigned)(size_t)L;
  int o1off = WS_O1 + ((((b & 6) + (hw >> 1)) * 8 + kvh) * 4 + (hw & 1) * 2) * 1024 + lane * 16;
  unsigned oiw = lb + L_OI + (2 * hw) * OI_STRIDE + lane * 16, oir = lb + L_OI + (c & 7) * OI_STRIDE + g * 16;
  int p2st = (c & 11) == 0 ? WS_P2 + ((((b & 6) + (c >> 2)) * 8 + kvh) * 448 + 64 * u2 + 16 * hw + 4 * g) * 2 : WS_END;
  int p2off = lane < 56 ? WS_P2 + (b * 8 + 2 * hw) * 896 + lane * 16 : WS_END;
  unsigned paw0 = lb + L_PA + lane * 256 + ((2 * hw) ^ (lane & 7)) * 16, paw1 = lb + L_PA + lane * 256 + ((2 * hw + 1) ^ (lane & 7)) * 16;
  unsigned par0 = lb + L_PA + g * 256 + (c ^ g) * 16, par1 = lb + L_PA + 1024 + g * 256 + (c ^ (4 + g)) * 16;
  unsigned h1w = lb + L_H1 + (g == 0 ? (16 * hw + c) * 2 : 128 + lane * 2), h1r = lb + L_H1 + g * 16;
  int p4st = c == 0 ? WS_P4 + ((b * 28 + 7 * hw) * 32 + j3) * P4S + g * 16 : WS_END;
  int p4a = WS_P4 + ((b * 28 + j3) * 32 + (lane >> 2)) * P4S + (lane & 3) * 16, p4b = lane < 48 ? p4a + 16 * P4S : WS_END;
  int oost = c < 4 ? (b * 448 + ocol) * 2 : 8 * 448 * 2;
  PIN(o1off); PIN(oiw); PIN(oir); PIN(p2st); PIN(p2off); PIN(paw0); PIN(paw1); PIN(par0); PIN(par1); PIN(h1w); PIN(h1r); PIN(p4st); PIN(p4a); PIN(p4b); PIN(oost);
  BARRIER();
  const unsigned tend = tstart + ((__builtin_amdgcn_readfirstlane((int)flags[4]) + T0_16 + 256 * TAU16) >> 4);
  if (u2 < 7) {
    v4u oa, ob;
    hold(D1, tend + TS1);
#pragma nounroll
    for (unsigned sp = 0; sp < SPIN_CAP; ++sp) {
      MEMFENCE();
      oa = __builtin_amdgcn_raw_buffer_load_b128(rw, o1off, 0, SC1);
      ob = __builtin_amdgcn_raw_buffer_load_b128(rw, o1off + 1024, 0, SC1);
      *LP(v4u, oiw) = oa;
      *LP(v4u, oiw + OI_STRIDE) = ob;
      __builtin_amdgcn_sched_barrier(0);
      if (!__any(umax4(ob, umax4(oa, 0u)) == SENT)) break;
    }
    BARRIER();
    v4u of[16];
#pragma unroll
    for (int t = 0; t < 16; ++t) of[t] = *LP(v4u, oir + t * 64);
    __builtin_amdgcn_sched_barrier(0);
    f32x4 d0 = {0.f, 0.f, 0.f, 0.f};
#pragma unroll
    for (int t = 0; t < 16; ++t) d0 = mfma16(wf[t], of[t], d0);
#pragma unroll
    for (int e = 0; e < 4; ++e) { d0[e] += dppf<XOR1>(d0[e]); d0[e] += dppf<XOR2>(d0[e]); }
    __builtin_amdgcn_raw_buffer_store_b64(v2u{nosent(cvtpk(d0[0], d0[1])), nosent(cvtpk(d0[2], d0[3]))}, rw, p2st, 0, SC1);
  }
  if (j3 < 28) {
    v4u f2[7][2];
    {
      LAS unsigned char* p2 = L + L_WA + (8 * g + (c >> 2)) * 896 + hw * 224 + (c & 3) * 8;
#pragma unroll
      for (int i = 0; i < 7; ++i) { f2[i][0] = tr_frag(p2 + i * 32, 896); f2[i][1] = tr_frag(p2 + 32 * 896 + i * 32, 896); }
      __builtin_amdgcn_sched_barrier(0);
    }
    {
      v4u pa, pb;
      hold(D2, tend + TS2);
#pragma nounroll
      for (unsigned sp = 0; sp < SPIN_CAP; ++sp) {
        MEMFENCE();
        pa = __builtin_amdgcn_raw_buffer_load_b128(rw, p2off, 0, SC1);
        pb = __builtin_amdgcn_raw_buffer_load_b128(rw, p2off + 896, 0, SC1);
        *LP(v4u, paw0) = pa;
        *LP(v4u, paw1) = pb;
        __builtin_amdgcn_sched_barrier(0);
        if (!__any(umax4(pb, umax4(pa, 0u)) == SENT)) break;
      }
    }
    BARRIER();
    {
      v4u ha[14];
#pragma unroll
      for (int t = 0; t < 14; ++t) ha[t] = *LP(v4u, ((t & 1) ? par1 : par0) + (t >> 1) * 2048);
      __builtin_amdgcn_sched_barrier(0);
      f32x4 gm = {0.f, 0.f, 0.f, 0.f}, d0 = {0.f, 0.f, 0.f, 0.f};
#pragma unroll
      for (int t = 0; t < 14; ++t) gm = mfma16(ha[t], ha[t], gm);
      asm volatile("" : "+v"(gm), "+v"(ha[0]));
#pragma unroll
      for (int t = 0; t < 14; ++t) d0 = mfma16(ha[t], f1[t], d0);
      float u = g < 2 ? (gm[0] + gm[1]) + (gm[2] + gm[3]) : gm[0];
      u = sw16_sum(u, u); u = sw32_sum(u, u);
      const float mean = dppf<BCAST + 9>(u) * (1.f / 448.f);
      const float msq = row_sum(c < 9 ? u : 0.f) * (1.f / 448.f);
      const float var = rbf(fmaxf(msq - mean * mean, 0.f));
      float mbc = rbf(mean) * csum, rden = __builtin_amdgcn_rcpf(rbf(__builtin_amdgcn_sqrtf(rbf(var + 1e-5f))));
#pragma unroll
      for (int q = 0; q < 14; ++q) { __builtin_amdgcn_sched_group_barrier(0x8, 1, 0); __builtin_amdgcn_sched_group_barrier(0x2, 3, 0); }
      asm volatile("" : "+v"(mbc), "+v"(rden), "+v"(d0));
      float v = g < 2 ? (d0[0] + d0[1]) + (d0[2] + d0[3]) : d0[0];
      v = sw16_sum(v, v); v = sw32_sum(v, v);
      float t = rbf(rbf((v - mbc) * rden) + b1v);
      t = t > 0.f ? t : 0.f;
      *LP(u16, h1w) = (u16)cvtpk(t * t, 0.f);
    }
    BARRIER();
    {
      const v4u ha = *LP(v4u, h1r), hb = *LP(v4u, h1r + 64);
      f32x4 d[7];
#pragma unroll
      for (int i = 0; i < 7; ++i) d[i] = mfma16(f2[i][0], ha, f32x4{0.f, 0.f, 0.f, 0.f});
      __builtin_amdgcn_sched_barrier(0);
#pragma unroll
      for (int i = 0; i < 9; ++i) {
        if (i < 7) d[i] = mfma16(f2[i][1], hb, d[i]);
        if (i >= 2) {
          const f32x4 e = d[i - 2];
          const v4u r = {nosent(__float_as_uint(e[0])), nosent(__float_as_uint(e[1])), nosent(__float_as_uint(e[2])), nosent(__float_as_uint(e[3]))};
          __builtin_amdgcn_raw_buffer_store_b128(r, rw, p4st, (i - 2) * 32 * P4S, SC1);
        }
        __builtin_amdgcn_sched_barrier(0);
      }
    }
    if (hw == 3) {
      float hres = 0.f;
#pragma unroll
      for (int r = 0; r < 8; ++r) hres += bf2f(*(LAS u16*)(L + L_PA + (ocol >> 3) * 256 + (r ^ ((ocol >> 3) & 7)) * 16 + (ocol & 7) * 2));
      hres = rbf(bf2f(*(LAS u16*)(L + L_PA + (ocol >> 3) * 256 + (8 ^ ((ocol >> 3) & 7)) * 16 + (ocol & 7) * 2)) + rbf(hres));
      v4u pa, pb;
      PIN(hres);
      hold(D3, tend);
#pragma nounroll
      for (unsigned sp = 0; sp < SPIN_CAP; ++sp) {
        MEMFENCE();
        pa = __builtin_amdgcn_raw_buffer_load_b128(rw, p4a, 0, SC1);
        pb = __builtin_amdgcn_raw_buffer_load_b128(rw, p4b, 0, SC1);
        if (!__any(umax4(pb, umax4(pa, 0u)) == SENT)) break;
      }
      const f32x4 t = __builtin_bit_cast(f32x4, pa) + __builtin_bit_cast(f32x4, pb);
      float v = sw16_sum(sw32_sum(t[0], t[2]), sw32_sum(t[1], t[3]));
      v += dppf<ROR4>(v); v += dppf<XOR8>(v);
      const float o = hres + rbf(rbf(v) + b2v);
      __builtin_amdgcn_raw_buffer_store_b16((u16)cvtpk(o, 0.f), __builtin_amdgcn_make_buffer_rsrc((void*)out, 0, 8 * 448 * 2, 0x00020000), oost, 0, 0);
    }
    if (hw == 1 && bid == 0 && lane == 0) __builtin_amdgcn_raw_buffer_store_b64(__builtin_bit_cast(v2u, tag), rw, WS_E, 0, SC1);
  }
}

extern "C" void kernel_launch(void* const* d_in, const int* in_sizes, int n_in, void* d_out, int out_size, void* d_ws, size_t ws_size, hipStream_t stream) {
  (void)in_sizes; (void)n_in; (void)out_size; (void)ws_size;
  fused_decode_block<<<256, 512, 0, stream>>>((unsigned char*)d_ws, (const u16*)d_in[0], (const u16*)d_in[3], (const u16*)d_in[1], (const u16*)d_in[2],
                                              (const u16*)d_in[4], (const u16*)d_in[6], (const u16*)d_in[8], (const u16*)d_in[7], (const u16*)d_in[9], (u16*)d_out);
}
```
